```python
import torch

def _softmax(x, axis=-1):
    return torch.softmax(x, dim=axis)
def _gelu(x):
    return torch.nn.functional.gelu(x, approximate="tanh")
def _silu(x):
    return torch.nn.functional.silu(x)
def _sigmoid(x):
    return torch.sigmoid(x)
def _layernorm(x, axis=-1, eps=1e-5):
    m = x.mean(dim=axis, keepdim=True)
    v = x.var(dim=axis, keepdim=True, unbiased=False)
    return (x - m) / torch.sqrt(v + eps)
def _scatter_add(tgt, idx, src, axis=0):
    return tgt.clone().index_add_(axis, idx, src)
def _segment_sum(src, seg, n):
    out = torch.zeros((n,) + src.shape[1:], dtype=src.dtype)
    return out.index_add_(0, seg, src)
def _pad(x, pw):
    flat = tuple(v for pair in reversed(pw) for v in pair)
    return torch.nn.functional.pad(x, flat)

kernel_name = 'sequential_preLN__mha_block_cached__geglu_ffn__decode'

def setup_inputs():

    _w = (lambda a: (lambda t: t.to(torch.bfloat16) if t.is_floating_point() else t)(torch.as_tensor(a)))
    def _A_setup():
        B, Sq, D = 1, 1, 512
        H, Sk, Dh = 32, 16384, 128
        return {
            "x": _w((lambda *s: torch.randn(*s, dtype=torch.bfloat16) * 0.05)(B, Sq, D)),
            "k_cache": _w((lambda *s: torch.randn(*s, dtype=torch.bfloat16) * 3.2)(B, H, Sk, Dh)),
            "v_cache": _w((lambda *s: torch.randn(*s, dtype=torch.bfloat16) * 0.4)(B, H, Sk, Dh)),
            "wq": _w((lambda *s: torch.randn(*s, dtype=torch.bfloat16) * 3.2)(D, H * Dh) * (D ** -0.5)),
            "wo": _w((lambda *s: torch.randn(*s, dtype=torch.bfloat16) * 0.4)(H * Dh, D) * ((H * Dh) ** -0.5)),
        }
    _A_in = _A_setup()

    def _B_setup():
        B, S, D, F = 1, 1, 512, 1792
        return {
            "x": _w((lambda *s: torch.randn(*s, dtype=torch.bfloat16) * 0.05)(B, S, D)),
            "w_gate": _w((lambda *s: torch.randn(*s, dtype=torch.bfloat16) * 0.05)(D, F)),
            "w_up": _w((lambda *s: torch.randn(*s, dtype=torch.bfloat16) * 0.05)(D, F)),
            "w_down": _w((lambda *s: torch.randn(*s, dtype=torch.bfloat16) * 0.05)(F, D)),
        }
    _B_in = _B_setup()

    return {
        "A_x": _A_in["x"],
        "A_k_cache": _A_in["k_cache"],
        "A_v_cache": _A_in["v_cache"],
        "A_wq": _A_in["wq"],
        "A_wo": _A_in["wo"],
        "B_x": _B_in["x"],
        "B_w_gate": _B_in["w_gate"],
        "B_w_up": _B_in["w_up"],
        "B_w_down": _B_in["w_down"],
    }

def reference(A_x, A_k_cache, A_v_cache, A_wq, A_wo, B_x, B_w_gate, B_w_up, B_w_down):
    def _A_ref(x, k_cache, v_cache, wq, wo):
        B, Sq, D = x.shape
        H, Dh = 32, 128
        q = (lambda x, *d: x.permute(*d))((x @ wq).reshape(B, Sq, H, Dh), 0, 2, 1, 3)
        scores = torch.einsum("bhqd,bhkd->bhqk", q, k_cache) * (Dh ** -0.5)
        p = _softmax(scores, axis=-1)
        o = torch.einsum("bhqk,bhkd->bhqd", p, v_cache)
        return (lambda x, *d: x.permute(*d))(o, 0, 2, 1, 3).reshape(B, Sq, H * Dh) @ wo

    def _B_ref(x, w_gate, w_up, w_down):
        return (_gelu(x @ w_gate) * (x @ w_up)) @ w_down

    x = A_x
    h = x + _A_ref(_layernorm(x, axis=-1), A_k_cache, A_v_cache, A_wq, A_wo)
    h2 = h + _B_ref(_layernorm(h, axis=-1), B_w_gate, B_w_up, B_w_down)
    return h2
```

```cpp
#include <hip/hip_runtime.h>
#include <hip/hip_bf16.h>
#include <stdint.h>

typedef __attribute__((ext_vector_type(4))) unsigned u32x4;
typedef __attribute__((ext_vector_type(2))) __bf16 bf16x2_t;
typedef __attribute__((ext_vector_type(4))) float f32x4;
typedef unsigned short bf16raw;
typedef unsigned long long u64;

constexpr int NH = 32, SK = 16384, DH = 128, DM = 512, FF = 1792;
constexpr int NSPLIT = 8;
constexpr int AW = 8;
constexpr int NT_ = AW * 64;
constexpr int NWG = NH * NSPLIT;
constexpr int TOK_PER_WG = SK / NSPLIT;
constexpr int TOK_PER_WAVE = TOK_PER_WG / AW;
constexpr int TT = 32;
constexpr int NL = TT / 4;
constexpr int NTILE = TOK_PER_WAVE / TT;
constexpr int REC = 132;

constexpr size_t WS_GEN = 0;
constexpr size_t WS_GA = 4096;
constexpr size_t WS_GML = WS_GA + (size_t)64 * 256 * 8 * 8;
constexpr size_t WS_GB = WS_GML + 256 * 136 * 8;
constexpr size_t WS_GC = WS_GB + 512 * 8;

constexpr int L_WO = 0;
constexpr int L_WG = L_WO + 16 * DM * 2;
constexpr int L_WU = L_WG + DM * 16;
constexpr int L_WD = L_WU + DM * 16;
constexpr int L_MISC = L_WD + FF * 4;
constexpr int L_TOTAL = L_MISC + 9216;
constexpr int MISC_FLOATS = (L_TOTAL - L_MISC) / 4;

constexpr float LOG2E = 1.4426950408889634f;
constexpr unsigned SPIN_MAX = 1u << 20;

__device__ __forceinline__ float bf_lo(unsigned u) { return __uint_as_float(u << 16); }
__device__ __forceinline__ float bf_hi(unsigned u) { return __uint_as_float(u & 0xffff0000u); }
__device__ __forceinline__ float rbf(float x) { return (float)(__bf16)x; }
__device__ __forceinline__ float dot2(unsigned k, unsigned q, float acc) {
  return __builtin_amdgcn_fdot2_f32_bf16(__builtin_bit_cast(bf16x2_t, k), __builtin_bit_cast(bf16x2_t, q), acc, false);
}
template <int CTRL> __device__ __forceinline__ float dpp(float x) {
  return __builtin_bit_cast(float, __builtin_amdgcn_mov_dpp(__builtin_bit_cast(int, x), CTRL, 0xf, 0xf, true));
}
__device__ __forceinline__ float row16_sum(float x) {
  x += dpp<0xB1>(x);
  x += dpp<0x4E>(x);
  x += dpp<0x141>(x);
  x += dpp<0x140>(x);
  return x;
}
__device__ __forceinline__ float wave_sum(float x) {
  x = row16_sum(x);
  x += __shfl_xor(x, 16);
  x += __shfl_xor(x, 32);
  return x;
}
__device__ __forceinline__ u32x4 ld_nt(const void* p) { return __builtin_nontemporal_load(reinterpret_cast<const u32x4*>(p)); }
__device__ __forceinline__ u32x4 ld16(const void* p) { return *reinterpret_cast<const u32x4*>(p); }

__device__ __forceinline__ void put_granule(u64* g, float v, unsigned tag) {
  __hip_atomic_store(g, ((u64)tag << 32) | (u64)__float_as_uint(v), __ATOMIC_RELAXED, __HIP_MEMORY_SCOPE_AGENT);
}
template <int N>
__device__ __forceinline__ void get_granules(const u64* const (&g)[N], unsigned tag, float (&v)[N]) {
  for (unsigned spins = 0; spins < SPIN_MAX; ++spins) {
    bool ok = true;
#pragma unroll
    for (int k = 0; k < N; ++k) {
      const u64 x = __hip_atomic_load(g[k], __ATOMIC_RELAXED, __HIP_MEMORY_SCOPE_AGENT);
      v[k] = __uint_as_float((unsigned)x);
      ok &= ((unsigned)(x >> 32) == tag);
    }
    if (ok) break;
    __builtin_amdgcn_s_sleep(2);
  }
}

__device__ __forceinline__ float gelu_tanh(float x) {
  const float k0 = 0.7978845608028654f, k1 = 0.044715f;
  const float inner = k0 * (x + k1 * x * x * x);
  return 0.5f * x * (1.f + tanhf(inner));
}

#define STAMP(i) do {} while (0)
__global__ void __launch_bounds__(NT_, 2)
k_block(const bf16raw* __restrict__ x, const bf16raw* __restrict__ kc, const bf16raw* __restrict__ vc,
        const bf16raw* __restrict__ wq, const bf16raw* __restrict__ wo, const bf16raw* __restrict__ wg,
        const bf16raw* __restrict__ wu, const bf16raw* __restrict__ wd, bf16raw* __restrict__ out,
        unsigned char* __restrict__ ws) {
  __shared__ __attribute__((aligned(16))) unsigned char smem[L_TOTAL];
  float* misc = reinterpret_cast<float*>(smem + L_MISC);
  const int tid = threadIdx.x, lane = tid & 63, wave = tid >> 6;
  const int r = lane >> 4, c = lane & 15;
  const int b = blockIdx.x;
  const int h = b & (NH - 1), sp = b / NH;
  const int xcd = b & 7, bi = b >> 3;

  const unsigned gen = __hip_atomic_load(reinterpret_cast<const unsigned*>(ws + WS_GEN), __ATOMIC_RELAXED, __HIP_MEMORY_SCOPE_AGENT);
  const unsigned tag = gen + 1u;
  STAMP(0);

  constexpr int RPT = DM / (AW * 4);
  const int kg = tid >> 4;
  u32x4 w[RPT];
  {
    const bf16raw* wp = wq + (size_t)(RPT * kg) * (NH * DH) + h * DH + 8 * c;
#pragma unroll
    for (int j = 0; j < RPT; ++j) w[j] = ld16(wp + (size_t)j * (NH * DH));
  }
  u32x4 xr[RPT / 8];
#pragma unroll
  for (int j = 0; j < RPT / 8; ++j) xr[j] = ld16(x + RPT * kg + 8 * j);
  const u32x4 xs = ld16(x + 8 * lane);
  __builtin_amdgcn_sched_barrier(0);

  const size_t tok0 = (size_t)h * SK + (size_t)sp * TOK_PER_WG + (size_t)wave * TOK_PER_WAVE;
  const bf16raw* kp = kc + (tok0 + r) * DH + 8 * c;
  const bf16raw* vp = vc + (tok0 + r) * DH + 8 * c;
  u32x4 kb[2][NL], vb[2][NL];
#pragma unroll
  for (int i = 0; i < NL; ++i) {
    kb[0][i] = ld_nt(kp + (size_t)(4 * i) * DH);
    vb[0][i] = ld_nt(vp + (size_t)(4 * i) * DH);
  }
  __builtin_amdgcn_sched_barrier(0);

  float mean_b, den;
  {
    float xv[8];
#pragma unroll
    for (int j = 0; j < 4; ++j) { xv[2 * j] = bf_lo(xs[j]); xv[2 * j + 1] = bf_hi(xs[j]); }
    float s1 = 0.f;
#pragma unroll
    for (int j = 0; j < 8; ++j) s1 += xv[j];
    s1 = wave_sum(s1);
    const float meanf = s1 * (1.0f / DM);
    float s2 = 0.f;
#pragma unroll
    for (int j = 0; j < 8; ++j) { const float d = xv[j] - meanf; s2 += d * d; }
    s2 = wave_sum(s2);
    mean_b = rbf(meanf);
    const float var_b = rbf(s2 * (1.0f / DM));
    den = rbf(sqrtf(rbf(var_b + 1e-5f)));
  }
  {
    float qa[8];
#pragma unroll
    for (int k = 0; k < 8; ++k) qa[k] = 0.f;
#pragma unroll
    for (int j = 0; j < RPT; ++j) {
      const float xj = (j & 1) ? bf_hi(xr[j >> 3][(j >> 1) & 3]) : bf_lo(xr[j >> 3][(j >> 1) & 3]);
      const float xn = rbf(rbf(xj - mean_b) / den);
#pragma unroll
      for (int k = 0; k < 4; ++k) {
        qa[2 * k] = fmaf(xn, bf_lo(w[j][k]), qa[2 * k]);
        qa[2 * k + 1] = fmaf(xn, bf_hi(w[j][k]), qa[2 * k + 1]);
      }
    }
#pragma unroll
    for (int k = 0; k < 8; ++k) { qa[k] += __shfl_xor(qa[k], 16); qa[k] += __shfl_xor(qa[k], 32); }
    if (r == 0) {
      *reinterpret_cast<f32x4*>(&misc[wave * 128 + 8 * c]) = f32x4{qa[0], qa[1], qa[2], qa[3]};
      *reinterpret_cast<f32x4*>(&misc[wave * 128 + 8 * c + 4]) = f32x4{qa[4], qa[5], qa[6], qa[7]};
    }
  }
  __syncthreads();
  unsigned qpk[4];
  {
    float qs[8];
#pragma unroll
    for (int k = 0; k < 8; ++k) qs[k] = 0.f;
#pragma unroll
    for (int ww = 0; ww < AW; ++ww) {
      const f32x4 a = *reinterpret_cast<const f32x4*>(&misc[ww * 128 + 8 * c]);
      const f32x4 bb = *reinterpret_cast<const f32x4*>(&misc[ww * 128 + 8 * c + 4]);
#pragma unroll
      for (int k = 0; k < 4; ++k) { qs[k] += a[k]; qs[4 + k] += bb[k]; }
    }
#pragma unroll
    for (int k = 0; k < 4; ++k) {
      const unsigned lo = __float_as_uint(rbf(qs[2 * k])) >> 16;
      const unsigned hi = __float_as_uint(rbf(qs[2 * k + 1])) & 0xffff0000u;
      qpk[k] = lo | hi;
    }
  }

  STAMP(1);
  const int gsl = xcd * 28 + bi;
  const bool has_gu = bi < 28;
  const int dcol = 64 * xcd + 2 * bi;
  const unsigned char* wo_src = reinterpret_cast<const unsigned char*>(wo) + (size_t)(h * DH + 16 * sp) * DM * 2 + tid * 16;

  const float SCALE = 0.08838834764831845f;
  float m = -INFINITY, l = 0.f;
  float acc[8];
#pragma unroll
  for (int k = 0; k < 8; ++k) acc[k] = 0.f;
  u32x4 st[2];
  u32x4 sg, su;
  unsigned sd[4];

#pragma unroll
  for (int t = 0; t < NTILE; ++t) {
    const int cur = t & 1, nxt = cur ^ 1;
    if (t > 0) {
      if (t == 1) {
        *reinterpret_cast<u32x4*>(smem + L_WO + tid * 16) = st[0];
        *reinterpret_cast<u32x4*>(smem + L_WO + NT_ * 16 + tid * 16) = st[1];
      }
      if (t == 1 && has_gu) {
        *reinterpret_cast<u32x4*>(smem + L_WG + tid * 16) = sg;
        *reinterpret_cast<u32x4*>(smem + L_WU + tid * 16) = su;
      }
      if (t == 2) {
#pragma unroll
        for (int j = 0; j < 4; ++j)
          if (tid + NT_ * j < FF) *reinterpret_cast<unsigned*>(smem + L_WD + (tid + NT_ * j) * 4) = sd[j];
      }
    }
    if (t + 1 < NTILE) {
#pragma unroll
      for (int i = 0; i < NL; ++i) {
        kb[nxt][i] = ld_nt(kp + (size_t)((t + 1) * TT + 4 * i) * DH);
        vb[nxt][i] = ld_nt(vp + (size_t)((t + 1) * TT + 4 * i) * DH);
      }
    }
    if (t == 0) {
      st[0] = ld16(wo_src);
      st[1] = ld16(wo_src + NT_ * 16);
    }
    if (t == 0 && has_gu) {
      sg = ld16(wg + (size_t)tid * FF + 8 * gsl);
      su = ld16(wu + (size_t)tid * FF + 8 * gsl);
    }
    if (t == 1) {
#pragma unroll
      for (int j = 0; j < 4; ++j)
        sd[j] = (tid + NT_ * j < FF) ? *reinterpret_cast<const unsigned*>(wd + (size_t)(tid + NT_ * j) * DM + dcol) : 0u;
    }
    __builtin_amdgcn_sched_barrier(0);

    float s[NL];
#pragma unroll
    for (int i = 0; i < NL; ++i) {
      float d = 0.f;
#pragma unroll
      for (int k = 0; k < 4; ++k) d = dot2(kb[cur][i][k], qpk[k], d);
      d = row16_sum(d);
      s[i] = rbf(rbf(d) * SCALE);
    }
    float tm = s[0];
#pragma unroll
    for (int i = 1; i < NL; ++i) tm = fmaxf(tm, s[i]);
    const float mn = fmaxf(m, tm);
    const float alpha = __builtin_amdgcn_exp2f((m - mn) * LOG2E);
    l *= alpha;
#pragma unroll
    for (int k = 0; k < 8; ++k) acc[k] *= alpha;
#pragma unroll
    for (int i = 0; i < NL; ++i) {
      const float p = __builtin_amdgcn_exp2f((s[i] - mn) * LOG2E);
      l += p;
#pragma unroll
      for (int k = 0; k < 4; ++k) {
        acc[2 * k] = fmaf(p, bf_lo(vb[cur][i][k]), acc[2 * k]);
        acc[2 * k + 1] = fmaf(p, bf_hi(vb[cur][i][k]), acc[2 * k + 1]);
      }
    }
    m = mn;
    __builtin_amdgcn_sched_barrier(0);
  }

  STAMP(2);
  {
    float M = fmaxf(m, __shfl_xor(m, 16));
    M = fmaxf(M, __shfl_xor(M, 32));
    const float sc = __builtin_amdgcn_exp2f((m - M) * LOG2E);
    l *= sc;
    l += __shfl_xor(l, 16); l += __shfl_xor(l, 32);
#pragma unroll
    for (int k = 0; k < 8; ++k) {
      acc[k] *= sc;
      acc[k] += __shfl_xor(acc[k], 16); acc[k] += __shfl_xor(acc[k], 32);
    }
    m = M;
  }
  __syncthreads();
  if (r == 0) {
    float* rec = &misc[wave * REC];
    *reinterpret_cast<f32x4*>(rec + 4 + 8 * c) = f32x4{acc[0], acc[1], acc[2], acc[3]};
    *reinterpret_cast<f32x4*>(rec + 4 + 8 * c + 4) = f32x4{acc[4], acc[5], acc[6], acc[7]};
    if (c == 0) { rec[0] = m; rec[1] = l; }
  }
  __syncthreads();

  u64* const GA = reinterpret_cast<u64*>(ws + WS_GA);
  u64* const GML = reinterpret_cast<u64*>(ws + WS_GML);
  u64* const GB = reinterpret_cast<u64*>(ws + WS_GB);
  u64* const GC = reinterpret_cast<u64*>(ws + WS_GC);
  const int p_self = h * NSPLIT + sp;

  {
    float wt[AW], M = -INFINITY;
#pragma unroll
    for (int ww = 0; ww < AW; ++ww) M = fmaxf(M, misc[ww * REC]);
    float L = 0.f;
#pragma unroll
    for (int ww = 0; ww < AW; ++ww) {
      wt[ww] = __builtin_amdgcn_exp2f((misc[ww * REC] - M) * LOG2E);
      L = fmaf(wt[ww], misc[ww * REC + 1], L);
    }
    if (tid < DH) {
      float a = 0.f;
#pragma unroll
      for (int ww = 0; ww < AW; ++ww) a = fmaf(wt[ww], misc[ww * REC + 4 + tid], a);
      put_granule(GML + (size_t)p_self * 136 + tid, a, tag);
    }
    if (tid == 128) put_granule(GML + (size_t)p_self * 136 + 128, M, tag);
    if (tid == 129) put_granule(GML + (size_t)p_self * 136 + 129, L, tag);
    float gv = 0.f;
    const int s2 = tid / 18, k2 = tid % 18;
    if (tid < 144) {
      const u64* gp1[1] = {GML + (size_t)(h * NSPLIT + s2) * 136 + (k2 < 16 ? 16 * sp + k2 : 112 + k2)};
      float v1[1];
      get_granules<1>(gp1, tag, v1);
      gv = v1[0];
    }
    __syncthreads();
    float* g1 = misc;
    float* o16 = misc + 160;
    if (tid < 144) g1[tid] = gv;
    __syncthreads();
    if (tid < 16) {
      float Mh = -INFINITY;
#pragma unroll
      for (int q = 0; q < 8; ++q) Mh = fmaxf(Mh, g1[q * 18 + 16]);
      float Lh = 0.f, o = 0.f;
#pragma unroll
      for (int q = 0; q < 8; ++q) {
        const float wq_ = __builtin_amdgcn_exp2f((g1[q * 18 + 16] - Mh) * LOG2E);
        Lh = fmaf(wq_, g1[q * 18 + 17], Lh);
        o = fmaf(wq_, g1[q * 18 + tid], o);
      }
      o16[tid] = rbf(o / Lh);
    }
    __syncthreads();
    float y = 0.f;
#pragma unroll
    for (int i = 0; i < 16; ++i)
      y = fmaf(o16[i], bf_lo(*reinterpret_cast<const bf16raw*>(smem + L_WO + i * (DM * 2) + tid * 2)), y);
    put_granule(GA + (size_t)p_self * DM + tid, y, tag);
  }

  STAMP(3);
  float* sA = misc;
  float* sB = misc + 1056;
  float* sC = misc + 1568;
  float* sD = misc + 2080;

  if (bi < 8) {
    const int ci = xcd * 8 + bi;
    const int p = tid >> 1, hf = tid & 1;
    const u64* ga0 = GA + (size_t)p * DM + 8 * ci + 4 * hf;
    const u64* gp[4] = {ga0, ga0 + 1, ga0 + 2, ga0 + 3};
    float v[4];
    get_granules<4>(gp, tag, v);
    __syncthreads();
    const float cf = 1.0f;
    float t4[4];
#pragma unroll
    for (int k = 0; k < 4; ++k) {
      float t = cf * v[k];
      t += __shfl_xor(t, 2); t += __shfl_xor(t, 4); t += __shfl_xor(t, 8);
      t += __shfl_xor(t, 16); t += __shfl_xor(t, 32);
      t4[k] = t;
    }
    if (lane < 2) {
#pragma unroll
      for (int k = 0; k < 4; ++k) sD[wave * 8 + 4 * lane + k] = t4[k];
    }
    __syncthreads();
    if (tid < 8) {
      float s = 0.f;
#pragma unroll
      for (int ww = 0; ww < AW; ++ww) s += sD[ww * 8 + tid];
      const int j = 8 * ci + tid;
      const float hval = rbf(bf_lo(x[j]) + rbf(s));
      put_granule(GB + j, hval, tag);
    }
  }

  STAMP(4);
  float hv;
  {
    const u64* gp[1] = {GB + tid};
    float v[1];
    get_granules<1>(gp, tag, v);
    hv = v[0];
  }
  STAMP(5);
  __syncthreads();
  sB[tid] = hv;
  if (has_gu) {
    float s1 = wave_sum(hv);
    if (lane == 0) sD[wave] = s1;
    __syncthreads();
    float meanf = 0.f;
#pragma unroll
    for (int ww = 0; ww < AW; ++ww) meanf += sD[ww];
    meanf *= (1.0f / DM);
    const float dlt = hv - meanf;
    float s2 = wave_sum(dlt * dlt);
    if (lane == 0) sD[8 + wave] = s2;
    __syncthreads();
    float varf = 0.f;
#pragma unroll
    for (int ww = 0; ww < AW; ++ww) varf += sD[8 + ww];
    const float var_b = rbf(varf * (1.0f / DM));
    const float hmean_b = rbf(meanf);
    const float hden = rbf(sqrtf(rbf(var_b + 1e-5f)));
    const float hn = rbf(rbf(hv - hmean_b) / hden);
    const u32x4 g8 = *reinterpret_cast<const u32x4*>(smem + L_WG + tid * 16);
    const u32x4 u8 = *reinterpret_cast<const u32x4*>(smem + L_WU + tid * 16);
    float ga[8], ua[8];
#pragma unroll
    for (int k = 0; k < 4; ++k) {
      ga[2 * k] = hn * bf_lo(g8[k]); ga[2 * k + 1] = hn * bf_hi(g8[k]);
      ua[2 * k] = hn * bf_lo(u8[k]); ua[2 * k + 1] = hn * bf_hi(u8[k]);
    }
#pragma unroll
    for (int k = 0; k < 8; ++k) { ga[k] = wave_sum(ga[k]); ua[k] = wave_sum(ua[k]); }
    if (lane == 0) {
#pragma unroll
      for (int k = 0; k < 8; ++k) { sD[16 + wave * 16 + k] = ga[k]; sD[16 + wave * 16 + 8 + k] = ua[k]; }
    }
    __syncthreads();
    if (tid < 8) {
      float g = 0.f, u = 0.f;
#pragma unroll
      for (int ww = 0; ww < AW; ++ww) { g += sD[16 + ww * 16 + tid]; u += sD[16 + ww * 16 + 8 + tid]; }
      g = rbf(g); u = rbf(u);
      put_granule(GC + 8 * gsl + tid, rbf(rbf(gelu_tanh(g)) * u), tag);
    }
  }

  STAMP(6);
  {
    const int i3 = (tid + 3 * NT_ < FF) ? tid + 3 * NT_ : tid;
    const u64* gp[4] = {GC + tid, GC + tid + NT_, GC + tid + 2 * NT_, GC + i3};
    float v[4];
    get_granules<4>(gp, tag, v);
    STAMP(7);
    float o0 = 0.f, o1 = 0.f;
#pragma unroll
    for (int j = 0; j < 4; ++j) {
      if (j < 3 || tid + 3 * NT_ < FF) {
        const unsigned wv = *reinterpret_cast<const unsigned*>(smem + L_WD + (tid + NT_ * j) * 4);
        o0 = fmaf(v[j], bf_lo(wv), o0);
        o1 = fmaf(v[j], bf_hi(wv), o1);
      }
    }
    o0 = wave_sum(o0); o1 = wave_sum(o1);
    __syncthreads();
    if (lane == 0) { sC[2 * wave] = o0; sC[2 * wave + 1] = o1; }
    __syncthreads();
    if (tid < 2) {
      float s = 0.f;
#pragma unroll
      for (int ww = 0; ww < AW; ++ww) s += sC[2 * ww + tid];
      const float o = rbf(sB[dcol + tid] + rbf(s));
      out[dcol + tid] = (bf16raw)(__float_as_uint(o) >> 16);
      if (tid == 0 && b == 0)
        __hip_atomic_store(reinterpret_cast<unsigned*>(ws + WS_GEN), tag, __ATOMIC_RELAXED, __HIP_MEMORY_SCOPE_AGENT);
    }
  }
}

extern "C" void kernel_launch(void* const* d_in, const int* in_sizes, int n_in,
                              void* d_out, int out_size, void* d_ws, size_t ws_size,
                              hipStream_t stream) {
  hipLaunchKernelGGL(k_block, dim3(NWG), dim3(NT_), 0, stream,
                     (const bf16raw*)d_in[0], (const bf16raw*)d_in[1], (const bf16raw*)d_in[2],
                     (const bf16raw*)d_in[3], (const bf16raw*)d_in[4], (const bf16raw*)d_in[6],
                     (const bf16raw*)d_in[7], (const bf16raw*)d_in[8], (bf16raw*)d_out,
                     (unsigned char*)d_ws);
}
```
